```python
import torch

def _softmax(x, axis=-1):
    return torch.softmax(x, dim=axis)
def _gelu(x):
    return torch.nn.functional.gelu(x, approximate="tanh")
def _silu(x):
    return torch.nn.functional.silu(x)
def _sigmoid(x):
    return torch.sigmoid(x)
def _layernorm(x, axis=-1, eps=1e-5):
    m = x.mean(dim=axis, keepdim=True)
    v = x.var(dim=axis, keepdim=True, unbiased=False)
    return (x - m) / torch.sqrt(v + eps)
def _scatter_add(tgt, idx, src, axis=0):
    return tgt.clone().index_add_(axis, idx, src)
def _segment_sum(src, seg, n):
    out = torch.zeros((n,) + src.shape[1:], dtype=src.dtype)
    return out.index_add_(0, seg, src)
def _pad(x, pw):
    flat = tuple(v for pair in reversed(pw) for v in pair)
    return torch.nn.functional.pad(x, flat)

kernel_name = 'standalone__kv_cache_quant_int4_int2_kv_with_per_channel_or_per_token_scales__sampling'

def setup_inputs():

    _w = (lambda a: (lambda t: t.to(torch.bfloat16) if t.is_floating_point() else t)(torch.as_tensor(a)))
    B = 32
    Sq = 4
    Skv = 2048
    Hq = 32
    Hkv = 16
    Dh = 128
    bits = 8
    G = 64
    if G <= 0 or G >= Skv:
        G = max(1, Skv // 2)
    Skv = (Skv // G) * G
    ngk = Skv // G
    qmax = (1 << bits) - 1
    return {
        "q": _w((lambda *s: torch.randn(*s, dtype=torch.bfloat16) * 0.2)(B, Hq, Sq, Dh)),
        "qk": _w(torch.randint(0, qmax + 1, (B, Hkv, Skv, Dh)).to(torch.int32)),
        "qv": _w(torch.randint(0, qmax + 1, (B, Hkv, Skv, Dh)).to(torch.int32)),
        "k_scales": _w(torch.abs((lambda *s: torch.randn(*s, dtype=torch.bfloat16) * 0.2)(B, Hkv, ngk, Dh)) * 0.05 + 0.02),
        "k_zeros": _w((lambda *s: torch.randn(*s, dtype=torch.bfloat16) * 0.2)(B, Hkv, ngk, Dh) + float(qmax) / 2.0),
        "v_scales": _w(torch.abs((lambda *s: torch.randn(*s, dtype=torch.bfloat16) * 0.05)(B, Hkv, Skv, 1)) * 0.05 + 0.02),
        "v_zeros": _w((lambda *s: torch.randn(*s, dtype=torch.bfloat16) * 0.05)(B, Hkv, Skv, 1) + float(qmax) / 2.0),
    }

def reference(q, qk, qv, k_scales, k_zeros, v_scales, v_zeros):
    B, Hq, Sq, Dh = q.shape
    Hkv, Skv = qk.shape[1], qk.shape[2]
    ngk = k_scales.shape[2]
    G = Skv // ngk
    ks = (lambda x, n, axis: torch.repeat_interleave(x, n, dim=axis))(k_scales, G, axis=2)
    kz = (lambda x, n, axis: torch.repeat_interleave(x, n, dim=axis))(k_zeros, G, axis=2)
    k = ((lambda x, dt: x.to(dt))(qk, q.dtype) - kz) * ks
    v = ((lambda x, dt: x.to(dt))(qv, q.dtype) - v_zeros) * v_scales
    rep = Hq // Hkv
    if rep > 1:
        k = (lambda x, n, axis: torch.repeat_interleave(x, n, dim=axis))(k, rep, axis=1)
        v = (lambda x, n, axis: torch.repeat_interleave(x, n, dim=axis))(v, rep, axis=1)
    scale = float(Dh) ** -0.5
    logits = torch.einsum("bhqd,bhkd->bhqk", q, k) * scale
    p = _softmax(logits, axis=-1)
    o = torch.einsum("bhqk,bhkd->bhqd", p, v)
    return o
```

```cpp
#include <hip/hip_runtime.h>
#include <hip/hip_bf16.h>
#include <stdint.h>

namespace {
constexpr int B = 32, SQ = 4, SKV = 2048, HQ = 32, HKV = 16, D = 128, GK = 64, NGK = SKV / GK;
constexpr int REP = HQ / HKV;
constexpr int M = REP * SQ;
constexpr int WAVES = 4, NTHR = WAVES * 64;
constexpr int TOK = 32;
constexpr int NSTEP = SKV / (TOK * WAVES);
constexpr int KROW = 272;
constexpr int KTILE = TOK * KROW;
static_assert(M == 8, "layout assumes 8 query rows per kv head");

using u32x2 = __attribute__((ext_vector_type(2))) unsigned;
using u32x4 = __attribute__((ext_vector_type(4))) unsigned;
using f32x4 = __attribute__((ext_vector_type(4))) float;
using bf16x2 = __attribute__((ext_vector_type(2))) __bf16;
using bf16x8 = __attribute__((ext_vector_type(8))) __bf16;
using rsrc_t = __amdgpu_buffer_rsrc_t;

__device__ __forceinline__ unsigned cvtpk(float lo, float hi) {
  bf16x2 v = {(__bf16)lo, (__bf16)hi};
  return __builtin_bit_cast(unsigned, v);
}
__device__ __forceinline__ float bflo(unsigned x) { return __builtin_bit_cast(float, x << 16); }
__device__ __forceinline__ float bfhi(unsigned x) { return __builtin_bit_cast(float, x & 0xffff0000u); }
__device__ __forceinline__ float i2f(unsigned x) { return (float)(int)x; }
__device__ __forceinline__ f32x4 mfma(u32x4 a, u32x4 b, f32x4 c) {
  return __builtin_amdgcn_mfma_f32_16x16x32_bf16(__builtin_bit_cast(bf16x8, a), __builtin_bit_cast(bf16x8, b), c, 0, 0, 0);
}
__device__ __forceinline__ float xh_max(float x) {
  auto s = __builtin_amdgcn_permlane16_swap(__float_as_uint(x), __float_as_uint(x), false, false);
  x = fmaxf(__uint_as_float(s[0]), __uint_as_float(s[1]));
  auto t = __builtin_amdgcn_permlane32_swap(__float_as_uint(x), __float_as_uint(x), false, false);
  return fmaxf(__uint_as_float(t[0]), __uint_as_float(t[1]));
}
__device__ __forceinline__ float xh_sum(float x) {
  auto s = __builtin_amdgcn_permlane16_swap(__float_as_uint(x), __float_as_uint(x), false, false);
  x = __uint_as_float(s[0]) + __uint_as_float(s[1]);
  auto t = __builtin_amdgcn_permlane32_swap(__float_as_uint(x), __float_as_uint(x), false, false);
  return __uint_as_float(t[0]) + __uint_as_float(t[1]);
}
__device__ __forceinline__ rsrc_t mkrsrc(const void* p, unsigned bytes) {
  return __builtin_amdgcn_make_buffer_rsrc(const_cast<void*>(p), 0, bytes, 0x00020000);
}
constexpr int AUX_NT = 2;

constexpr int LDS_BYTES = WAVES * KTILE;

__global__ void __launch_bounds__(NTHR, 2)
kvq_attn_kernel(const uint16_t* __restrict__ q, const int* __restrict__ qk, const int* __restrict__ qv,
                const uint16_t* __restrict__ ks, const uint16_t* __restrict__ kz,
                const uint16_t* __restrict__ vs, const uint16_t* __restrict__ vz, uint16_t* __restrict__ out) {
  __shared__ __attribute__((aligned(16))) unsigned char lds[LDS_BYTES];
  const int pair = blockIdx.x;
  const int tid = threadIdx.x, lane = tid & 63;
  const int w = __builtin_amdgcn_readfirstlane(tid >> 6);
  const int c = lane & 15, h = lane >> 4;
  constexpr float C2 = 0.088388347648318440f * 1.4426950408889634f;

  const rsrc_t rk = mkrsrc(qk + (size_t)pair * (SKV * D), SKV * D * 4);
  const rsrc_t rv = mkrsrc(qv + (size_t)pair * (SKV * D), SKV * D * 4);
  const rsrc_t rks = mkrsrc(ks + (size_t)pair * (NGK * D), NGK * D * 2);
  const rsrc_t rkz = mkrsrc(kz + (size_t)pair * (NGK * D), NGK * D * 2);
  const rsrc_t rvs = mkrsrc(vs + (size_t)pair * SKV, SKV * 2);
  const rsrc_t rvz = mkrsrc(vz + (size_t)pair * SKV, SKV * 2);
  const int kvo = lane * 16;
  const int vvo = h * (8 * D * 4) + c * 16;
  const int svo = (lane & 31) * 8;
  const int tvo = h * 16;

  u32x4 kr[16], vr[16];
  u32x2 ksr, kzr;
  u32x4 vsr, vzr;
  auto load_k = [&](int s) {
    const int tok0 = (s * WAVES + w) * TOK;
    const int g = tok0 / GK;
    ksr = __builtin_amdgcn_raw_buffer_load_b64(rks, svo, g * (D * 2), 0);
    kzr = __builtin_amdgcn_raw_buffer_load_b64(rkz, svo, g * (D * 2), 0);
#pragma unroll
    for (int i = 0; i < 16; ++i)
      kr[i] = __builtin_amdgcn_raw_buffer_load_b128(rk, kvo + (i & 3) * 1024, tok0 * (D * 4) + (i >> 2) * 4096, AUX_NT);
  };
  auto load_v = [&](int s) {
    const int tok0 = (s * WAVES + w) * TOK;
    vsr = __builtin_amdgcn_raw_buffer_load_b128(rvs, tvo, tok0 * 2, 0);
    vzr = __builtin_amdgcn_raw_buffer_load_b128(rvz, tvo, tok0 * 2, 0);
#pragma unroll
    for (int i = 0; i < 16; ++i)
      vr[i] = __builtin_amdgcn_raw_buffer_load_b128(rv, vvo + (i >> 1) * (D * 4) + (i & 1) * 256, tok0 * (D * 4), AUX_NT);
  };

  u32x4 qf[4];
#pragma unroll
  for (int s = 0; s < 4; ++s)
    qf[s] = *reinterpret_cast<const u32x4*>(q + ((size_t)pair * M + (c & 7)) * D + 32 * s + 8 * h);
  load_k(0);
  load_v(0);

  unsigned char* kt = lds + w * KTILE;
  unsigned char* kwr = kt + (lane >> 5) * KROW + (lane & 31) * 8;
  const unsigned char* krd = kt + c * KROW + h * 16;

  f32x4 acc[8], accb;
#pragma unroll
  for (int i = 0; i < 8; ++i) acc[i] = f32x4{0.f, 0.f, 0.f, 0.f};
  accb = f32x4{0.f, 0.f, 0.f, 0.f};
  float m = -INFINITY, l = 0.f;

  auto step = [&](int s, bool prefetch) {
    {
      const float s0 = bflo(ksr[0]), s1 = bfhi(ksr[0]), s2 = bflo(ksr[1]), s3 = bfhi(ksr[1]);
      const float z0 = bflo(kzr[0]), z1 = bfhi(kzr[0]), z2 = bflo(kzr[1]), z3 = bfhi(kzr[1]);
#pragma unroll
      for (int i = 0; i < 16; ++i) {
        const float a0 = (i2f(kr[i][0]) - z0) * s0, a1 = (i2f(kr[i][1]) - z1) * s1;
        const float a2 = (i2f(kr[i][2]) - z2) * s2, a3 = (i2f(kr[i][3]) - z3) * s3;
        const int rho = 16 * ((i >> 1) & 1) + 4 * (i >> 2) + 2 * (i & 1);
        *reinterpret_cast<u32x2*>(kwr + rho * KROW) = u32x2{cvtpk(a0, a1), cvtpk(a2, a3)};
      }
    }
    __builtin_amdgcn_sched_barrier(0);
    if (prefetch) load_k(s + 1);
    __builtin_amdgcn_sched_barrier(0);
    f32x4 sc[2];
#pragma unroll
    for (int T = 0; T < 2; ++T) {
      sc[T] = f32x4{0.f, 0.f, 0.f, 0.f};
#pragma unroll
      for (int st = 0; st < 4; ++st) {
        const u32x4 a = *reinterpret_cast<const u32x4*>(krd + T * 16 * KROW + st * 64);
        sc[T] = mfma(a, qf[st], sc[T]);
      }
    }
    float x[8];
#pragma unroll
    for (int j = 0; j < 8; ++j) x[j] = sc[j >> 2][j & 3] * C2;
    float tm = fmaxf(fmaxf(fmaxf(x[0], x[1]), fmaxf(x[2], x[3])), fmaxf(fmaxf(x[4], x[5]), fmaxf(x[6], x[7])));
    tm = xh_max(tm);
    const float mn = fmaxf(m, tm);
    const float alpha = __builtin_amdgcn_exp2f(m - mn);
    m = mn;
    float p[8], ps = 0.f;
#pragma unroll
    for (int j = 0; j < 8; ++j) { p[j] = __builtin_amdgcn_exp2f(x[j] - mn); ps += p[j]; }
    l = l * alpha + ps;
    u32x4 pb;
#pragma unroll
    for (int j = 0; j < 4; ++j) pb[j] = cvtpk(p[2 * j] * bflo(vsr[j]), p[2 * j + 1] * bfhi(vsr[j]));
#pragma unroll
    for (int i = 0; i < 8; ++i) acc[i] *= alpha;
    accb *= alpha;
    accb = mfma(vzr, pb, accb);
    __builtin_amdgcn_sched_barrier(0);
#pragma unroll
    for (int half = 0; half < 2; ++half)
#pragma unroll
      for (int cm = 0; cm < 4; ++cm) {
        u32x4 va;
#pragma unroll
        for (int jj = 0; jj < 4; ++jj)
          va[jj] = cvtpk(i2f(vr[(2 * jj) * 2 + half][cm]), i2f(vr[(2 * jj + 1) * 2 + half][cm]));
        acc[half * 4 + cm] = mfma(va, pb, acc[half * 4 + cm]);
      }
    __builtin_amdgcn_sched_barrier(0);
    if (prefetch) load_v(s + 1);
    __builtin_amdgcn_sched_barrier(0);
  };
  for (int s = 0; s < NSTEP - 1; ++s) step(s, true);
  step(NSTEP - 1, false);

  l = xh_sum(l);
  __syncthreads();
  float* mg = reinterpret_cast<float*>(lds);
  if (c < 8) {
    float* rec = mg + (w * 8 + c) * 132;
    if (h == 0) { rec[0] = m; rec[1] = l; }
#pragma unroll
    for (int half = 0; half < 2; ++half)
#pragma unroll
      for (int r = 0; r < 4; ++r) {
        f32x4 o;
#pragma unroll
        for (int cm = 0; cm < 4; ++cm) o[cm] = acc[half * 4 + cm][r] - accb[r];
        *reinterpret_cast<f32x4*>(rec + 4 + 64 * half + 16 * h + 4 * r) = o;
      }
  }
  __syncthreads();
  {
    const int row = tid >> 5, d0 = (tid & 31) * 4;
    float mm = -INFINITY;
#pragma unroll
    for (int ww = 0; ww < WAVES; ++ww) mm = fmaxf(mm, mg[(ww * 8 + row) * 132]);
    float L = 0.f;
    f32x4 o = {0.f, 0.f, 0.f, 0.f};
#pragma unroll
    for (int ww = 0; ww < WAVES; ++ww) {
      const float* rec = mg + (ww * 8 + row) * 132;
      const float e = __builtin_amdgcn_exp2f(rec[0] - mm);
      L += e * rec[1];
      o += e * *reinterpret_cast<const f32x4*>(rec + 4 + d0);
    }
    const float inv = 1.f / L;
    *reinterpret_cast<u32x2*>(out + ((size_t)pair * M + row) * D + d0) = u32x2{cvtpk(o[0] * inv, o[1] * inv), cvtpk(o[2] * inv, o[3] * inv)};
  }
}
}

extern "C" void kernel_launch(void* const* d_in, const int* in_sizes, int n_in, void* d_out, int out_size, void* d_ws, size_t ws_size,
                              hipStream_t stream) {
  (void)in_sizes; (void)n_in; (void)out_size; (void)d_ws; (void)ws_size;
  kvq_attn_kernel<<<dim3(B * HKV), dim3(NTHR), 0, stream>>>(
      (const uint16_t*)d_in[0], (const int*)d_in[1], (const int*)d_in[2], (const uint16_t*)d_in[3], (const uint16_t*)d_in[4],
      (const uint16_t*)d_in[5], (const uint16_t*)d_in[6], (uint16_t*)d_out);
}
```

```cpp
#include <hip/hip_runtime.h>
#include <hip/hip_bf16.h>
#include <stdint.h>

namespace {
constexpr int B = 32, SQ = 4, SKV = 2048, HQ = 32, HKV = 16, D = 128, GK = 64, NGK = SKV / GK;
constexpr int REP = HQ / HKV;
constexpr int M = REP * SQ;
constexpr int WAVES = 8, NTHR = WAVES * 64;
constexpr int TOK = 32;
constexpr int NST = SKV / (TOK * WAVES);
constexpr int NPH = 2;
constexpr int KROW = 272;
constexpr int KTILE = TOK * KROW;
static_assert(M == 8 && B * HKV == 256 * NPH, "layout assumes 8 query rows per kv head and 512 pairs");
constexpr unsigned TAU = 1742;
constexpr int CROSS = 20;
constexpr unsigned LEAD = 100;

using u32x2 = __attribute__((ext_vector_type(2))) unsigned;
using u32x4 = __attribute__((ext_vector_type(4))) unsigned;
using f32x2 = __attribute__((ext_vector_type(2))) float;
using f32x4 = __attribute__((ext_vector_type(4))) float;
using f32x16 = __attribute__((ext_vector_type(16))) float;
using bf16x2 = __attribute__((ext_vector_type(2))) __bf16;
using bf16x8 = __attribute__((ext_vector_type(8))) __bf16;
using rsrc_t = __amdgpu_buffer_rsrc_t;

__device__ __forceinline__ unsigned cvtpk(float lo, float hi) {
  bf16x2 v = {(__bf16)lo, (__bf16)hi};
  return __builtin_bit_cast(unsigned, v);
}
__device__ __forceinline__ float bflo(unsigned x) { return __builtin_bit_cast(float, x << 16); }
__device__ __forceinline__ float bfhi(unsigned x) { return __builtin_bit_cast(float, x & 0xffff0000u); }
__device__ __forceinline__ float i2f(unsigned x) { return (float)(int)x; }
__device__ __forceinline__ f32x4 mfma16(u32x4 a, u32x4 b, f32x4 c) {
  return __builtin_amdgcn_mfma_f32_16x16x32_bf16(__builtin_bit_cast(bf16x8, a), __builtin_bit_cast(bf16x8, b), c, 0, 0, 0);
}
__device__ __forceinline__ f32x16 mfma32(u32x4 a, u32x4 b, f32x16 c) {
  return __builtin_amdgcn_mfma_f32_32x32x16_bf16(__builtin_bit_cast(bf16x8, a), __builtin_bit_cast(bf16x8, b), c, 0, 0, 0);
}
__device__ __forceinline__ float xh_max(float x) {
  auto s = __builtin_amdgcn_permlane16_swap(__float_as_uint(x), __float_as_uint(x), false, false);
  x = fmaxf(__uint_as_float(s[0]), __uint_as_float(s[1]));
  auto t = __builtin_amdgcn_permlane32_swap(__float_as_uint(x), __float_as_uint(x), false, false);
  return fmaxf(__uint_as_float(t[0]), __uint_as_float(t[1]));
}
__device__ __forceinline__ float xh_sum(float x) {
  auto s = __builtin_amdgcn_permlane16_swap(__float_as_uint(x), __float_as_uint(x), false, false);
  x = __uint_as_float(s[0]) + __uint_as_float(s[1]);
  auto t = __builtin_amdgcn_permlane32_swap(__float_as_uint(x), __float_as_uint(x), false, false);
  return __uint_as_float(t[0]) + __uint_as_float(t[1]);
}
__device__ __forceinline__ rsrc_t mkrsrc(const void* p, unsigned bytes) {
  return __builtin_amdgcn_make_buffer_rsrc(const_cast<void*>(p), 0, bytes, 0x00020000);
}
constexpr int AUX_NT = 2;

constexpr int SCB = 2 * NGK * D * 2 + 2 * SKV * 2;
constexpr int O_KZ = NGK * D * 2, O_VS = 2 * NGK * D * 2, O_VZ = O_VS + SKV * 2;
constexpr int PREC = 544;
constexpr int L_KT = 0;
constexpr int L_SC = L_KT + WAVES * KTILE;
constexpr int L_P = L_SC + NPH * SCB;
constexpr int L_Q = L_P + WAVES * NST * PREC;
constexpr int LDS_BYTES = L_Q + NPH * M * D * 2;
static_assert(LDS_BYTES <= 163840 && WAVES * M * 132 * 4 <= WAVES * KTILE, "LDS budget");

__device__ __forceinline__ void load_burst(u32x4 (&r)[16], const int* kbase, const int* vbase, int w, int lane, int n, int rot) {
  const bool isv = (n & NST) != 0;
  const rsrc_t rs = mkrsrc(isv ? vbase : kbase, 9 * SKV * D * 4);
  const int so = ((n >> 4) & 1) * (8 * SKV * D * 4) + (((n + rot) & (NST - 1)) * WAVES + w) * (TOK * D * 4);
  int vo = lane * 16 + (n >= 4 * NST ? 0x40000000 : 0);
  asm volatile("" : "+v"(vo));
#pragma unroll
  for (int i = 0; i < 16; ++i) r[i] = __builtin_amdgcn_raw_buffer_load_b128(rs, vo + (i & 3) * 1024, so + (i >> 2) * 4096, AUX_NT);
}

__global__ void __launch_bounds__(NTHR, 2)
kvq_attn_kernel(const uint16_t* __restrict__ q, const int* __restrict__ qk, const int* __restrict__ qv,
                const uint16_t* __restrict__ ks, const uint16_t* __restrict__ kz,
                const uint16_t* __restrict__ vs, const uint16_t* __restrict__ vz, uint16_t* __restrict__ out) {
  const unsigned long long T0 = __builtin_amdgcn_s_memrealtime();
  __shared__ __attribute__((aligned(16))) unsigned char lds[LDS_BYTES];
  const int cu = blockIdx.x;
  const int pair0 = (cu & 7) + 16 * (cu >> 3);
  const int tid = threadIdx.x, lane = tid & 63;
  const int w = __builtin_amdgcn_readfirstlane(tid >> 6);
  const int c = lane & 15, h = lane >> 4;
  constexpr float C2 = 0.088388347648318440f * 1.4426950408889634f;

  const unsigned slot = (unsigned)((cu >> 3) ^ (cu >> 6)) & 7u;
  const int rot = (int)slot;
  const unsigned tb = (unsigned)T0 + LEAD;
  const int sgn = (int)((unsigned)__builtin_amdgcn_s_getreg(20 | (3 << 11)) & 1u) * 2 - 1;
  auto pace = [&](int n) {
    const unsigned u = (unsigned)n * 64u + ((n == 0 || n == 4 * NST - 1) ? 4u : 8u) * slot;
    const int nn = n <= 2 * NST ? n : 4 * NST - n;
    const unsigned tgt = tb + (unsigned)((int)(u * TAU) - sgn * nn * 64 * CROSS) / 256u;
#pragma clang loop unroll(disable)
    for (int it = 0; it < 8192; ++it) {
      const unsigned now = (unsigned)__builtin_amdgcn_s_memrealtime();
      if ((int)(now - tgt) >= 0) break;
      __builtin_amdgcn_s_sleep(1);
    }
  };

  u32x4 ra[16], rb[16];
  const int* kbase = qk + (size_t)pair0 * (SKV * D);
  const int* vbase = qv + (size_t)pair0 * (SKV * D);

  {
    u32x4 t[NPH][3], tq;
#pragma unroll
    for (int ph = 0; ph < NPH; ++ph) {
      const size_t p = (size_t)(pair0 + 8 * ph);
      t[ph][0] = reinterpret_cast<const u32x4*>(ks + p * (NGK * D))[tid];
      t[ph][1] = reinterpret_cast<const u32x4*>(kz + p * (NGK * D))[tid];
      t[ph][2] = reinterpret_cast<const u32x4*>((tid < 256 ? vs : vz) + p * SKV)[tid & 255];
    }
    tq = reinterpret_cast<const u32x4*>(q + (size_t)(pair0 + 8 * ((tid >> 7) & 1)) * (M * D))[tid & 127];
    __builtin_amdgcn_sched_barrier(0);
    pace(0);
    load_burst(ra, kbase, vbase, w, lane, 0, rot);
    __builtin_amdgcn_sched_barrier(0);
    const int e = tid & 3;
    const int voff = O_VS + (tid >> 8) * (SKV * 2) + ((tid & 255) >> 2) * 64 + (e >> 1) * 32 + (e & 1) * 8;
#pragma unroll
    for (int ph = 0; ph < NPH; ++ph) {
      unsigned char* s = lds + L_SC + ph * SCB;
      *reinterpret_cast<u32x4*>(s + tid * 16) = t[ph][0];
      *reinterpret_cast<u32x4*>(s + O_KZ + tid * 16) = t[ph][1];
      const u32x4 x = t[ph][2];
      *reinterpret_cast<u32x2*>(s + voff) = u32x2{(x[0] & 0xffffu) | (x[1] << 16), (x[2] & 0xffffu) | (x[3] << 16)};
      *reinterpret_cast<u32x2*>(s + voff + 16) = u32x2{(x[0] >> 16) | (x[1] & 0xffff0000u), (x[2] >> 16) | (x[3] & 0xffff0000u)};
    }
    if (tid < 256) *reinterpret_cast<u32x4*>(lds + L_Q + tid * 16) = tq;
    __syncthreads();
  }

  unsigned char* kt = lds + L_KT + w * KTILE;
  unsigned char* kwr = kt + (lane >> 5) * (4 * KROW) + (lane & 31) * 8;
  const unsigned char* krd = kt + c * KROW + h * 16;
  unsigned char* prec = lds + L_P + w * (NST * PREC);

  for (int ph = 0; ph < NPH; ++ph) {
    const unsigned char* scl = lds + L_SC + ph * SCB;
    u32x4 qf[4];
#pragma unroll
    for (int s = 0; s < 4; ++s) qf[s] = *reinterpret_cast<const u32x4*>(lds + L_Q + ph * (M * D * 2) + (c & 7) * (D * 2) + s * 64 + h * 16);
    float m = -INFINITY, l = 0.f;
    f32x4 accb = {0.f, 0.f, 0.f, 0.f};

    auto proc_k = [&](const u32x4 (&r)[16], int st) __attribute__((always_inline)) {
      const int tok0 = (((st + rot) & (NST - 1)) * WAVES + w) * TOK;
      {
        const u32x2 ksr = *reinterpret_cast<const u32x2*>(scl + (tok0 / GK) * (D * 2) + (lane & 31) * 8);
        const u32x2 kzr = *reinterpret_cast<const u32x2*>(scl + O_KZ + (tok0 / GK) * (D * 2) + (lane & 31) * 8);
        const float s0 = bflo(ksr[0]), s1 = bfhi(ksr[0]), s2 = bflo(ksr[1]), s3 = bfhi(ksr[1]);
        const float z0 = bflo(kzr[0]), z1 = bfhi(kzr[0]), z2 = bflo(kzr[1]), z3 = bfhi(kzr[1]);
#pragma unroll
        for (int i = 0; i < 16; ++i) {
          const float a0 = (i2f(r[i][0]) - z0) * s0, a1 = (i2f(r[i][1]) - z1) * s1;
          const float a2 = (i2f(r[i][2]) - z2) * s2, a3 = (i2f(r[i][3]) - z3) * s3;
          const int rho = 16 * ((i >> 2) & 1) + 8 * (i >> 3) + (i & 3);
          *reinterpret_cast<u32x2*>(kwr + rho * KROW) = u32x2{cvtpk(a0, a1), cvtpk(a2, a3)};
        }
      }
      f32x4 sc[2];
#pragma unroll
      for (int T = 0; T < 2; ++T) {
        sc[T] = f32x4{0.f, 0.f, 0.f, 0.f};
#pragma unroll
        for (int s = 0; s < 4; ++s) {
          const u32x4 a = *reinterpret_cast<const u32x4*>(krd + T * 16 * KROW + s * 64);
          sc[T] = mfma16(a, qf[s], sc[T]);
        }
      }
      const u32x4 vsr = *reinterpret_cast<const u32x4*>(scl + O_VS + tok0 * 2 + h * 16);
      const u32x4 vzr = *reinterpret_cast<const u32x4*>(scl + O_VZ + tok0 * 2 + h * 16);
      float x[8];
#pragma unroll
      for (int j = 0; j < 8; ++j) x[j] = sc[j >> 2][j & 3] * C2;
      float tm = fmaxf(fmaxf(fmaxf(x[0], x[1]), fmaxf(x[2], x[3])), fmaxf(fmaxf(x[4], x[5]), fmaxf(x[6], x[7])));
      tm = xh_max(tm);
      const float mn = fmaxf(m, tm);
      const float alpha = __builtin_amdgcn_exp2f(m - mn);
      m = mn;
      float p[8], ps = 0.f;
#pragma unroll
      for (int j = 0; j < 8; ++j) { p[j] = __builtin_amdgcn_exp2f(x[j] - mn); ps += p[j]; }
      l = l * alpha + ps;
      u32x4 pb;
#pragma unroll
      for (int j = 0; j < 4; ++j) pb[j] = cvtpk(p[2 * j] * bflo(vsr[j]), p[2 * j + 1] * bfhi(vsr[j]));
      accb *= alpha;
      accb = mfma16(vzr, pb, accb);
      if (c < 8) {
        *reinterpret_cast<u32x4*>(prec + st * PREC + (h * 8 + c) * 16) = pb;
        if (h == 0) *reinterpret_cast<float*>(prec + st * PREC + 512 + c * 4) = alpha;
      }
    };
#pragma clang loop unroll(disable)
    for (int st = 0; st < NST; st += 2) {
      const int n = ph * 2 * NST + st;
      pace(n + 1);
      load_burst(rb, kbase, vbase, w, lane, n + 1, rot);
      __builtin_amdgcn_sched_barrier(0);
      proc_k(ra, st);
      __builtin_amdgcn_sched_barrier(0);
      pace(n + 2);
      load_burst(ra, kbase, vbase, w, lane, n + 2, rot);
      __builtin_amdgcn_sched_barrier(0);
      proc_k(rb, st + 1);
      __builtin_amdgcn_sched_barrier(0);
    }

    __syncthreads();
    f32x16 acc[4];
#pragma unroll
    for (int i = 0; i < 4; ++i)
#pragma unroll
      for (int k = 0; k < 16; ++k) acc[i][k] = 0.f;
    auto proc_v = [&](const u32x4 (&r)[16], int st) __attribute__((always_inline)) {
      const float alpha = *reinterpret_cast<const float*>(prec + st * PREC + 512 + (lane & 7) * 4);
#pragma unroll
      for (int cm = 0; cm < 4; ++cm) acc[cm] *= alpha;
#pragma unroll
      for (int ib = 0; ib < 2; ++ib) {
        const u32x4 pb = *reinterpret_cast<const u32x4*>(prec + st * PREC + ((2 * ib + (lane >> 5)) * 8 + (lane & 7)) * 16);
#pragma unroll
        for (int cm = 0; cm < 4; ++cm) {
          u32x4 va;
#pragma unroll
          for (int jj = 0; jj < 4; ++jj) va[jj] = cvtpk(i2f(r[8 * ib + 2 * jj][cm]), i2f(r[8 * ib + 2 * jj + 1][cm]));
          acc[cm] = mfma32(va, pb, acc[cm]);
        }
      }
    };
#pragma clang loop unroll(disable)
    for (int st = 0; st < NST; st += 2) {
      const int n = ph * 2 * NST + NST + st;
      pace(n + 1);
      load_burst(rb, kbase, vbase, w, lane, n + 1, rot);
      __builtin_amdgcn_sched_barrier(0);
      proc_v(ra, st);
      __builtin_amdgcn_sched_barrier(0);
      if (n + 2 < 4 * NST) pace(n + 2);
      load_burst(ra, kbase, vbase, w, lane, n + 2, rot);
      __builtin_amdgcn_sched_barrier(0);
      proc_v(rb, st + 1);
      __builtin_amdgcn_sched_barrier(0);
    }
    {
      const float ls = xh_sum(l);
      float* mg = reinterpret_cast<float*>(lds + L_KT);
      if ((lane & 31) < 8) {
        float* rec = mg + (w * 8 + (lane & 7)) * 132;
        if (lane < 8) { rec[0] = m; rec[1] = ls; }
        const float bias = accb[0];
#pragma unroll
        for (int k = 0; k < 16; ++k) {
          const f32x4 o = {acc[0][k] - bias, acc[1][k] - bias, acc[2][k] - bias, acc[3][k] - bias};
          *reinterpret_cast<f32x4*>(rec + 4 + 4 * ((k & 3) + 8 * (k >> 2)) + 16 * (lane >> 5)) = o;
        }
      }
      __syncthreads();
      {
        const int row = w, d0 = lane * 2;
        float mm = -INFINITY;
#pragma unroll
        for (int ww = 0; ww < WAVES; ++ww) mm = fmaxf(mm, mg[(ww * 8 + row) * 132]);
        float L = 0.f;
        f32x2 o = {0.f, 0.f};
#pragma unroll
        for (int ww = 0; ww < WAVES; ++ww) {
          const float* rec = mg + (ww * 8 + row) * 132;
          const float e = __builtin_amdgcn_exp2f(rec[0] - mm);
          L += e * rec[1];
          o += e * *reinterpret_cast<const f32x2*>(rec + 4 + d0);
        }
        const float inv = 1.f / L;
        *reinterpret_cast<unsigned*>(out + ((size_t)(pair0 + 8 * ph) * M + row) * D + d0) = cvtpk(o[0] * inv, o[1] * inv);
      }
      if (ph + 1 < NPH) __syncthreads();
    }
  }
}
}

extern "C" void kernel_launch(void* const* d_in, const int* in_sizes, int n_in, void* d_out, int out_size, void* d_ws, size_t ws_size,
                              hipStream_t stream) {
  (void)in_sizes; (void)n_in; (void)out_size; (void)d_ws; (void)ws_size;
  kvq_attn_kernel<<<dim3(256), dim3(NTHR), 0, stream>>>(
      (const uint16_t*)d_in[0], (const int*)d_in[1], (const int*)d_in[2], (const uint16_t*)d_in[3], (const uint16_t*)d_in[4],
      (const uint16_t*)d_in[5], (const uint16_t*)d_in[6], (uint16_t*)d_out);
}
```
